```python
import torch

def _softmax(x, axis=-1):
    return torch.softmax(x, dim=axis)
def _gelu(x):
    return torch.nn.functional.gelu(x, approximate="tanh")
def _silu(x):
    return torch.nn.functional.silu(x)
def _sigmoid(x):
    return torch.sigmoid(x)
def _layernorm(x, axis=-1, eps=1e-5):
    m = x.mean(dim=axis, keepdim=True)
    v = x.var(dim=axis, keepdim=True, unbiased=False)
    return (x - m) / torch.sqrt(v + eps)
def _scatter_add(tgt, idx, src, axis=0):
    return tgt.clone().index_add_(axis, idx, src)
def _segment_sum(src, seg, n):
    out = torch.zeros((n,) + src.shape[1:], dtype=src.dtype)
    return out.index_add_(0, seg, src)
def _pad(x, pw):
    flat = tuple(v for pair in reversed(pw) for v in pair)
    return torch.nn.functional.pad(x, flat)

kernel_name = 'standalone__instancenorm__sampling'

def setup_inputs():

    _w = (lambda a: (lambda t: t.to(torch.bfloat16) if t.is_floating_point() else t)(torch.as_tensor(a)))
    B, C, H, W = 32, 512, 32, 32
    return {
        "x": _w(abs((lambda *s: torch.randn(*s, dtype=torch.bfloat16) * 0.05)(B, C, H, W))),
        "g": _w((lambda *s: torch.randn(*s, dtype=torch.bfloat16) * 0.05)(C) + 1.0),
        "b": _w((lambda *s: torch.randn(*s, dtype=torch.bfloat16) * 0.4)(C)),
    }

def reference(x, g, b):
    mu = x.mean(axis=(-2, -1), keepdims=True)
    var = ((x - mu) ** 2).mean(axis=(-2, -1), keepdims=True)
    xhat = (x - mu) / torch.sqrt(var + 1e-5)
    return xhat * g.reshape(1, -1, 1, 1) + b.reshape(1, -1, 1, 1)
```

```cpp
#include <hip/hip_runtime.h>
#include <hip/hip_bf16.h>
#include <stdint.h>

typedef uint32_t u32x4 __attribute__((ext_vector_type(4)));

static constexpr int kHW = 1024;
static constexpr float kEps = 1e-5f;

__device__ __forceinline__ float bf_lo(uint32_t u) { return __uint_as_float(u << 16); }
__device__ __forceinline__ float bf_hi(uint32_t u) { return __uint_as_float(u & 0xffff0000u); }

template <int CTRL, int ROWMASK>
__device__ __forceinline__ float dpp_add(float v) {
    int t = __builtin_amdgcn_update_dpp(0, __float_as_int(v), CTRL, ROWMASK, 0xf, true);
    return v + __int_as_float(t);
}

__device__ __forceinline__ float wave_sum(float v) {
    v = dpp_add<0xb1, 0xf>(v);
    v = dpp_add<0x4e, 0xf>(v);
    v = dpp_add<0x141, 0xf>(v);
    v = dpp_add<0x140, 0xf>(v);
    v = dpp_add<0x142, 0xa>(v);
    v = dpp_add<0x143, 0xc>(v);
    return __int_as_float(__builtin_amdgcn_readlane(__float_as_int(v), 63));
}

__device__ __forceinline__ uint32_t pack_bf16(float a, float b) {
    typedef float f32x2 __attribute__((ext_vector_type(2)));
    typedef __bf16 bf16x2 __attribute__((ext_vector_type(2)));
    f32x2 f = {a, b};
    bf16x2 h = __builtin_convertvector(f, bf16x2);
    return __builtin_bit_cast(uint32_t, h);
}

__global__ __launch_bounds__(256) void instnorm_kernel(const u32x4* __restrict__ x,
                                                       const uint16_t* __restrict__ g,
                                                       const uint16_t* __restrict__ b,
                                                       u32x4* __restrict__ out, int rows, int C) {
    const int lane = threadIdx.x & 63;
    const int row = __builtin_amdgcn_readfirstlane(blockIdx.x * 4 + (threadIdx.x >> 6));
    if (row >= rows) return;
    const u32x4* xr = x + (size_t)row * (kHW / 8);
    u32x4 v0 = xr[lane];
    u32x4 v1 = xr[lane + 64];
    const int c = row % C;
    const float gg = __uint_as_float((uint32_t)g[c] << 16);
    const float bb = __uint_as_float((uint32_t)b[c] << 16);

    float f[16];
#pragma unroll
    for (int j = 0; j < 4; ++j) {
        f[2 * j] = bf_lo(v0[j]);
        f[2 * j + 1] = bf_hi(v0[j]);
        f[8 + 2 * j] = bf_lo(v1[j]);
        f[8 + 2 * j + 1] = bf_hi(v1[j]);
    }
    float s = 0.f;
#pragma unroll
    for (int j = 0; j < 16; ++j) s += f[j];
    const float mu = wave_sum(s) * (1.0f / kHW);
    float q = 0.f;
#pragma unroll
    for (int j = 0; j < 16; ++j) {
        f[j] -= mu;
        q += f[j] * f[j];
    }
    const float var = wave_sum(q) * (1.0f / kHW);
    const float sc = gg * rsqrtf(var + kEps);
    u32x4 o0, o1;
#pragma unroll
    for (int j = 0; j < 4; ++j) {
        o0[j] = pack_bf16(f[2 * j] * sc + bb, f[2 * j + 1] * sc + bb);
        o1[j] = pack_bf16(f[8 + 2 * j] * sc + bb, f[8 + 2 * j + 1] * sc + bb);
    }
    u32x4* orow = out + (size_t)row * (kHW / 8);
    orow[lane] = o0;
    orow[lane + 64] = o1;
}

extern "C" void kernel_launch(void* const* d_in, const int* in_sizes, int n_in, void* d_out,
                              int out_size, void* d_ws, size_t ws_size, hipStream_t stream) {
    const int C = in_sizes[1];
    const int rows = in_sizes[0] / kHW;
    instnorm_kernel<<<(rows + 3) / 4, 256, 0, stream>>>(
        (const u32x4*)d_in[0], (const uint16_t*)d_in[1], (const uint16_t*)d_in[2], (u32x4*)d_out,
        rows, C);
}
```

```cpp
#include <hip/hip_runtime.h>
#include <hip/hip_bf16.h>
#include <stdint.h>


typedef uint32_t u32x4 __attribute__((ext_vector_type(4)));
typedef uint32_t u32x2 __attribute__((ext_vector_type(2)));
typedef float f32x2 __attribute__((ext_vector_type(2)));
typedef __bf16 bf16x2 __attribute__((ext_vector_type(2)));

static constexpr int kHW = 1024;
static constexpr int kRowBytes = kHW * 2;
static constexpr float kEps = 1e-5f;
static constexpr int kLoads = 8;
static constexpr int kBlock = 1024;
static constexpr int kWaves = kBlock / 64;
static constexpr int kRowsPerBlock = 4 * kWaves;
static constexpr int kAuxNt = 2;
static constexpr int kAuxSc1 = 16;

__device__ __forceinline__ float bf_lo(uint32_t u) { return __uint_as_float(u << 16); }
__device__ __forceinline__ float bf_hi(uint32_t u) { return __uint_as_float(u & 0xffff0000u); }

template <int CTRL>
__device__ __forceinline__ float dpp_add(float v) {
    const int t = __builtin_amdgcn_update_dpp(0, __float_as_int(v), CTRL, 0xf, 0xf, true);
    return v + __int_as_float(t);
}

__device__ __forceinline__ float row16_sum(float v) {
    v = dpp_add<0xb1>(v);
    v = dpp_add<0x4e>(v);
    v = dpp_add<0x141>(v);
    v = dpp_add<0x140>(v);
    return v;
}

__device__ __forceinline__ uint32_t pack_bf16(f32x2 f) {
    return __builtin_bit_cast(uint32_t, __builtin_convertvector(f, bf16x2));
}

__device__ __forceinline__ void normalise(u32x4 (&v)[kLoads], float gamma, float beta,
                                          const f32x2 (&f)[kLoads * 4], float var) {
    const float sc = gamma * __builtin_amdgcn_rsqf(var + kEps);
#pragma unroll
    for (int i = 0; i < kLoads; ++i)
#pragma unroll
        for (int j = 0; j < 4; ++j) v[i][j] = pack_bf16(f[i * 4 + j] * sc + beta);
}

__device__ __forceinline__ float centre(const u32x4 (&v)[kLoads], f32x2 (&f)[kLoads * 4]) {
    f32x2 sa[2] = {{0.f, 0.f}, {0.f, 0.f}};
#pragma unroll
    for (int i = 0; i < kLoads; ++i)
#pragma unroll
        for (int j = 0; j < 4; ++j) {
            f[i * 4 + j] = f32x2{bf_lo(v[i][j]), bf_hi(v[i][j])};
            sa[j & 1] += f[i * 4 + j];
        }
    const f32x2 s2 = sa[0] + sa[1];
    const float mu = row16_sum(s2.x + s2.y) * (1.0f / kHW);
    f32x2 qa[4] = {{0.f, 0.f}, {0.f, 0.f}, {0.f, 0.f}, {0.f, 0.f}};
#pragma unroll
    for (int k = 0; k < kLoads * 4; ++k) {
        f[k] -= mu;
        qa[k & 3] += f[k] * f[k];
    }
    const f32x2 q2 = (qa[0] + qa[1]) + (qa[2] + qa[3]);
    return row16_sum(q2.x + q2.y) * (1.0f / kHW);
}

template <int NBLK, int C>
__global__ __launch_bounds__(kBlock) void instnorm_kernel(const void* __restrict__ x,
                                                          const u32x2* __restrict__ g,
                                                          const u32x2* __restrict__ b,
                                                          void* __restrict__ out) {
    static_assert(C % 4 == 0, "a wave's four rows must share one 8-byte parameter word pair");
    static_assert((NBLK & (NBLK - 1)) == 0, "the band rotation wraps with a mask");
    constexpr int kBytes = NBLK * kRowsPerBlock * kRowBytes;
    const int lane = threadIdx.x & 63;
    const unsigned wave = __builtin_amdgcn_readfirstlane(threadIdx.x >> 6);
    const unsigned row0 = (wave * NBLK + ((blockIdx.x + wave * 16) & (NBLK - 1))) * 4;
    const int sub = lane >> 4;
    const int voff = sub * kRowBytes + (lane & 15) * 16;
    const auto rx = __builtin_amdgcn_make_buffer_rsrc((void*)x, 0, kBytes, 0x00020000);
    const auto ro = __builtin_amdgcn_make_buffer_rsrc(out, 0, kBytes, 0x00020000);

    __builtin_amdgcn_s_setprio(3);
    __builtin_amdgcn_sched_barrier(0);
    u32x4 v[kLoads];
#pragma unroll
    for (int i = 0; i < kLoads; ++i)
        v[i] = __builtin_amdgcn_raw_buffer_load_b128(rx, voff + i * 256, row0 * kRowBytes, kAuxNt);
    __builtin_amdgcn_sched_barrier(0);
    __builtin_amdgcn_s_setprio(0);

    const u32x2 gw = g[(row0 % C) / 4];
    const u32x2 bw = b[(row0 % C) / 4];
    __builtin_amdgcn_sched_barrier(0);

    f32x2 f[kLoads * 4];
    const float var = centre(v, f);

    uint32_t g0 = gw.x, g1 = gw.y, b0 = bw.x, b1 = bw.y;
    asm volatile("" : "+s"(g0), "+s"(g1), "+s"(b0), "+s"(b1));
    const uint32_t gu = (sub & 2) ? g1 : g0;
    const uint32_t bu = (sub & 2) ? b1 : b0;
    normalise(v, (sub & 1) ? bf_hi(gu) : bf_lo(gu), (sub & 1) ? bf_hi(bu) : bf_lo(bu), f, var);

#pragma unroll
    for (int i = 0; i < kLoads; ++i) asm volatile("" : "+v"(v[i]));
    __builtin_amdgcn_sched_barrier(0);
    const int obase = voff + row0 * kRowBytes;
#pragma unroll
    for (int i = 0; i < kLoads; ++i)
        __builtin_amdgcn_raw_buffer_store_b128(v[i], ro, obase + i * 256, 0, kAuxSc1);
}

__global__ __launch_bounds__(256) void instnorm_generic_kernel(const u32x4* __restrict__ x,
                                                               const uint16_t* __restrict__ g,
                                                               const uint16_t* __restrict__ b,
                                                               u32x4* __restrict__ out, int rows,
                                                               int channels) {
    const int row = blockIdx.x * 16 + (threadIdx.x >> 4);
    const int rr = row < rows ? row : rows - 1;
    const size_t base = (size_t)rr * (kHW / 8) + (threadIdx.x & 15);
    u32x4 v[kLoads];
#pragma unroll
    for (int i = 0; i < kLoads; ++i) v[i] = x[base + i * 16];
    const int c = rr % channels;
    const float gamma = __uint_as_float((uint32_t)g[c] << 16);
    const float beta = __uint_as_float((uint32_t)b[c] << 16);
    f32x2 f[kLoads * 4];
    const float var = centre(v, f);
    normalise(v, gamma, beta, f, var);
    if (row < rows) {
#pragma unroll
        for (int i = 0; i < kLoads; ++i) out[base + i * 16] = v[i];
    }
}

extern "C" void kernel_launch(void* const* d_in, const int* in_sizes, int n_in, void* d_out,
                              int out_size, void* d_ws, size_t ws_size, hipStream_t stream) {
    const int rows = in_sizes[0] / kHW;
    const int channels = in_sizes[1];
    if (rows == 256 * kRowsPerBlock && channels == 512) {
        instnorm_kernel<256, 512><<<256, kBlock, 0, stream>>>(d_in[0], (const u32x2*)d_in[1],
                                                              (const u32x2*)d_in[2], d_out);
    } else if (rows > 0) {
        instnorm_generic_kernel<<<(rows + 15) / 16, 256, 0, stream>>>(
            (const u32x4*)d_in[0], (const uint16_t*)d_in[1], (const uint16_t*)d_in[2],
            (u32x4*)d_out, rows, channels);
    }
}
```
